```python
import torch

def _softmax(x, axis=-1):
    return torch.softmax(x, dim=axis)
def _gelu(x):
    return torch.nn.functional.gelu(x, approximate="tanh")
def _silu(x):
    return torch.nn.functional.silu(x)
def _sigmoid(x):
    return torch.sigmoid(x)
def _layernorm(x, axis=-1, eps=1e-5):
    m = x.mean(dim=axis, keepdim=True)
    v = x.var(dim=axis, keepdim=True, unbiased=False)
    return (x - m) / torch.sqrt(v + eps)
def _scatter_add(tgt, idx, src, axis=0):
    return tgt.clone().index_add_(axis, idx, src)
def _segment_sum(src, seg, n):
    out = torch.zeros((n,) + src.shape[1:], dtype=src.dtype)
    return out.index_add_(0, seg, src)
def _pad(x, pw):
    flat = tuple(v for pair in reversed(pw) for v in pair)
    return torch.nn.functional.pad(x, flat)

kernel_name = 'interleave_layers__sliding_block__glu_gated_bilinear__prompt'

def setup_inputs():

    _w = (lambda a: (lambda t: t.to(torch.bfloat16) if t.is_floating_point() else t)(torch.as_tensor(a)))
    def _A_setup():
        B, S, D = 1, 2048, 448
        return {
            "x": _w((lambda *s: torch.randn(*s, dtype=torch.bfloat16) * 0.05)(B, S, D)),
            "wqkv": _w((lambda *s: torch.randn(*s, dtype=torch.bfloat16) * 0.05)(D, 3 * D)),
            "wo": _w((lambda *s: torch.randn(*s, dtype=torch.bfloat16) * 0.05)(D, D)),
        }
    _A_in = _A_setup()

    def _B_setup():
        B, S, D, F = 1, 2048, 448, 1792
        return {
            "x": _w((lambda *s: torch.randn(*s, dtype=torch.bfloat16) * 0.05)(B, S, D)),
            "w_gate": _w((lambda *s: torch.randn(*s, dtype=torch.bfloat16) * 0.8)(D, F) * (D ** -0.5)),
            "w_up": _w((lambda *s: torch.randn(*s, dtype=torch.bfloat16) * 0.8)(D, F) * (D ** -0.5)),
            "w_down": _w((lambda *s: torch.randn(*s, dtype=torch.bfloat16) * 0.8)(F, D) * (F ** -0.5)),
        }
    _B_in = _B_setup()

    return {
        "A_x": _A_in["x"],
        "A_wqkv": _A_in["wqkv"],
        "A_wo": _A_in["wo"],
        "B_x": _B_in["x"],
        "B_w_gate": _B_in["w_gate"],
        "B_w_up": _B_in["w_up"],
        "B_w_down": _B_in["w_down"],
    }

def reference(A_x, A_wqkv, A_wo, B_x, B_w_gate, B_w_up, B_w_down):
    def _A_ref(x, wqkv, wo):
        B, S, D = x.shape
        H = 16; Dh = D // H; W = 256
        qkv = (lambda x, *d: x.permute(*d))((x @ wqkv).reshape(B, S, 3, H, Dh), 2, 0, 3, 1, 4)
        q, k, v = qkv[0], qkv[1], qkv[2]
        scores = torch.einsum("bhqd,bhkd->bhqk", q, k) * (Dh ** -0.5)
        idx = torch.arange(S)
        mask = (idx[:, None] - idx[None, :] >= 0) & (idx[:, None] - idx[None, :] < W)
        scores = torch.where(mask[None, None], scores, -1e9)
        p = _softmax(scores, axis=-1)
        o = torch.einsum("bhqk,bhkd->bhqd", p, v)
        return (lambda x, *d: x.permute(*d))(o, 0, 2, 1, 3).reshape(B, S, D) @ wo

    def _B_ref(x, w_gate, w_up, w_down):
        g = torch.einsum("bsd,df->bsf", x, w_gate)
        u = torch.einsum("bsd,df->bsf", x, w_up)
        h = g * u
        return torch.einsum("bsf,fd->bsd", h, w_down)

    x = A_x
    x = x + _A_ref(_layernorm(x, axis=-1), A_wqkv, A_wo)
    x = x + _B_ref(_layernorm(x, axis=-1), B_w_gate, B_w_up, B_w_down)
    x = x + _A_ref(_layernorm(x, axis=-1), A_wqkv, A_wo)
    x = x + _B_ref(_layernorm(x, axis=-1), B_w_gate, B_w_up, B_w_down)
    return x
```

```cpp
#include <hip/hip_runtime.h>
#include <hip/hip_bf16.h>

#define GAS __attribute__((address_space(1)))
#define LAS __attribute__((address_space(3)))
typedef unsigned v4u __attribute__((ext_vector_type(4)));
typedef unsigned v2u __attribute__((ext_vector_type(2)));
typedef float f32x4 __attribute__((ext_vector_type(4)));
typedef float f32x2 __attribute__((ext_vector_type(2)));
typedef float f32x16 __attribute__((ext_vector_type(16)));
typedef __bf16 bf16x8 __attribute__((ext_vector_type(8)));
typedef __bf16 bf16x2 __attribute__((ext_vector_type(2)));


namespace {
constexpr int SEQ = 2048, DM = 448, NH = 16, DH = 28, FF = 1792;
constexpr int KB = 1024;
constexpr int KS_D = 28;
constexpr int KS_O = 32;
constexpr int KS_F = 112;
constexpr int CTRL_OFF = 0, CTRL_BYTES = 192 * KB;
constexpr int WQKV_OFF = CTRL_OFF + CTRL_BYTES;
constexpr int WO_OFF = WQKV_OFF + 16 * 84 * KB;
constexpr int WGU_OFF = WO_OFF + 448 * KB;
constexpr int WD_OFF = WGU_OFF + 3136 * KB;
constexpr int CSQ_OFF = WD_OFF + 1568 * KB;
constexpr int CSG_OFF = CSQ_OFF + 42 * KB;
constexpr int X0_OFF = CSG_OFF + 98 * KB;
constexpr int XS = 64 * 28 * KB;
constexpr int XB = XS + 64 * 14 * 256;
constexpr int LAYER_OFF = X0_OFF + XB;
constexpr int KV_L = 0;
constexpr int O_L = KV_L + 16 * 64 * 4 * KB;
constexpr int X1_L = O_L + 64 * 32 * KB;
constexpr int H_L = X1_L + XB;
constexpr int X2_L = H_L + 64 * 112 * KB;
constexpr int LAYER_BYTES = X2_L + XB;
constexpr int WS_BYTES = LAYER_OFF + 2 * LAYER_BYTES;
constexpr int W_TMO = 0;
constexpr int W_KV = 32;
constexpr int W_OF = W_KV + 2 * 256 * 32;
constexpr int W_X1 = W_OF + 2 * 128 * 32;
constexpr int W_HF = W_X1 + 2 * 64 * 32;
constexpr int W_X2 = W_HF + 2 * 128 * 32;
constexpr int W_WT = W_X2 + 64 * 32;
constexpr int W_END = W_WT + 28 * 32;
static_assert(W_END * 4 <= CTRL_BYTES, "control block");
constexpr int L_A = 0;
constexpr int L_KV = 84 * KB;
constexpr int L_ST = 132 * KB;
constexpr int L_CS = L_ST + 2 * KB;
constexpr int L_RT = L_CS + 2 * KB;
constexpr int L_BYTES = L_RT + 24 * KB;
constexpr unsigned kSpinCap = 1u << 18;
constexpr int AUX_SC1 = 16;
constexpr int RSRC_W3 = 0x00020000;

__device__ __forceinline__ float as_f(unsigned u) { return __builtin_bit_cast(float, u); }
__device__ __forceinline__ unsigned as_u(float f) { return __builtin_bit_cast(unsigned, f); }
__device__ __forceinline__ bf16x2 as_b2(unsigned u) { return __builtin_bit_cast(bf16x2, u); }
__device__ __forceinline__ unsigned pack2(float a, float b) {
  f32x2 v = {a, b};
  bf16x2 r = __builtin_convertvector(v, bf16x2);
  return __builtin_bit_cast(unsigned, r);
}
__device__ __forceinline__ float lo_f(unsigned u) { return as_f(u << 16); }
__device__ __forceinline__ float hi_f(unsigned u) { return as_f(u & 0xffff0000u); }
__device__ __forceinline__ f32x16 mfma(v4u a, v4u b, f32x16 c) {
  return __builtin_amdgcn_mfma_f32_32x32x16_bf16(__builtin_bit_cast(bf16x8, a), __builtin_bit_cast(bf16x8, b), c, 0, 0, 0);
}
__device__ __forceinline__ void halves(float s, float& lo, float& hi) {
  auto r = __builtin_amdgcn_permlane32_swap(as_u(s), as_u(s), false, false);
  unsigned a = r[0], b = r[1];
  lo = as_f(a); hi = as_f(b);
}
__device__ __forceinline__ v4u ld_sc1(__amdgpu_buffer_rsrc_t rs, int voff, int soff) {
  return __builtin_amdgcn_raw_buffer_load_b128(rs, voff, soff, AUX_SC1);
}
__device__ __forceinline__ v4u ld_pl(__amdgpu_buffer_rsrc_t rs, int voff, int soff) {
  return __builtin_amdgcn_raw_buffer_load_b128(rs, voff, soff, 0);
}
__device__ __forceinline__ void st_sc1(v4u v, __amdgpu_buffer_rsrc_t rs, int voff, int soff) {
  __builtin_amdgcn_raw_buffer_store_b128(v, rs, voff, soff, AUX_SC1);
}
__device__ __forceinline__ v4u ldf_sc1(__amdgpu_buffer_rsrc_t rs, int l16, int base, int i) { return ld_sc1(rs, l16 + (i & 3) * KB, base + (i >> 2) * 4 * KB); }
__device__ __forceinline__ v4u ldf_pl(__amdgpu_buffer_rsrc_t rs, int l16, int base, int i) { return ld_pl(rs, l16 + (i & 3) * KB, base + (i >> 2) * 4 * KB); }
__device__ __forceinline__ void stf_sc1(v4u v, __amdgpu_buffer_rsrc_t rs, int l16, int base, int i) { st_sc1(v, rs, l16 + (i & 3) * KB, base + (i >> 2) * 4 * KB); }
__device__ __forceinline__ int opq(int v) { asm volatile("" : "+s"(v)); return v; }
template <int I> struct IC { static constexpr int v = I; };
template <int B, int E, class F> __device__ __forceinline__ void sfor(F&& f) {
  if constexpr (E - B == 1) f(IC<B>{});
  else if constexpr (E - B > 1) { sfor<B, (B + E) / 2>(f); sfor<(B + E) / 2, E>(f); }
}
#define SFOR(i, B, E) sfor<B, E>([&](auto ic_##i) __attribute__((always_inline)) { constexpr int i = decltype(ic_##i)::v;
#define SEND });
#define DRAIN() asm volatile("s_waitcnt vmcnt(0)" ::: "memory")
#define SCHED() __builtin_amdgcn_sched_barrier(0)
__device__ __forceinline__ void wg_barrier() {
  asm volatile("s_waitcnt lgkmcnt(0)" ::: "memory");
  __builtin_amdgcn_s_barrier();
  asm volatile("" ::: "memory");
}

__device__ __forceinline__ bool wait_flags_v(__amdgpu_buffer_rsrc_t rs, int word, int vo) {
  const int so = opq(CTRL_OFF + word * 4);
  for (unsigned spins = 0;;) {
    asm volatile("" ::: "memory");
    const unsigned v = __builtin_amdgcn_raw_buffer_load_b32(rs, vo, so, AUX_SC1);
    if (__builtin_amdgcn_ballot_w64(v == 0u) == 0ull) break;
    if (++spins > kSpinCap) { __builtin_amdgcn_raw_buffer_store_b32(1u, rs, 0, CTRL_OFF + W_TMO * 4, AUX_SC1); return false; }
    if ((spins & 255u) == 0u && __builtin_amdgcn_readfirstlane(__builtin_amdgcn_raw_buffer_load_b32(rs, 0, CTRL_OFF + W_TMO * 4, AUX_SC1)) != 0u) return false;
    __builtin_amdgcn_s_sleep(2);
  }
  asm volatile("" ::: "memory");
  __builtin_amdgcn_fence(__ATOMIC_ACQUIRE, "wavefront");
  return true;
}
__device__ __forceinline__ bool wait_flags(__amdgpu_buffer_rsrc_t rs, int word, int n, int lane) { return wait_flags_v(rs, word, lane < n ? lane * 4 : 0); }
__device__ __forceinline__ void raise_flag(__amdgpu_buffer_rsrc_t rs, int word, int lane) {
  asm volatile("" ::: "memory");
  if (lane == 0) __builtin_amdgcn_raw_buffer_store_b32(1u, rs, 0, opq(CTRL_OFF + word * 4), AUX_SC1);
  asm volatile("" ::: "memory");
}
__device__ __forceinline__ void publish(__amdgpu_buffer_rsrc_t rs, int word, int lane) { DRAIN(); raise_flag(rs, word, lane); }

__device__ __forceinline__ f32x2 tile_stats(v4u f0, v4u f1) {
  unsigned u[8] = {f0.x, f0.y, f0.z, f0.w, f1.x, f1.y, f1.z, f1.w};
  float v[16], s = 0.f;
#pragma unroll
  for (int i = 0; i < 8; ++i) { v[2 * i] = lo_f(u[i]); v[2 * i + 1] = hi_f(u[i]); s += v[2 * i] + v[2 * i + 1]; }
  float a, b; halves(s, a, b); s = a + b;
  const float m = s * (1.f / 32.f);
  float q = 0.f;
#pragma unroll
  for (int i = 0; i < 16; ++i) { const float d = v[i] - m; q += d * d; }
  halves(q, a, b);
  return f32x2{s, a + b};
}

struct PrepArgs { const unsigned short *x, *wqkv, *wo; unsigned char* ws; };
constexpr int V_GU = 2 * 28 * 7, V_WD = 7 * 28, V_WO = 7 * 8, V_QKV = 48 * 7, V_X = 64 * 7;
constexpr int PREP_UNITS = (V_QKV + V_X + V_WO) / 4;
__device__ __forceinline__ unsigned plo(unsigned a, unsigned b) { return __builtin_amdgcn_perm(b, a, 0x05040100u); }
__device__ __forceinline__ unsigned phi(unsigned a, unsigned b) { return __builtin_amdgcn_perm(b, a, 0x07060302u); }
__device__ __forceinline__ float sum8(v4u f) {
  unsigned x = f.x, y = f.y, z = f.z, w = f.w;
  float s = ((lo_f(x) + hi_f(x)) + (lo_f(y) + hi_f(y))) + ((lo_f(z) + hi_f(z)) + (lo_f(w) + hi_f(w)));
  s += __shfl_xor(s, 8); s += __shfl_xor(s, 16); s += __shfl_xor(s, 32);
  return s;
}

__global__ void __launch_bounds__(256) k_prep(PrepArgs a) {
  const int tid = threadIdx.x, lane = tid & 63;
  const int blk = blockIdx.x;
  if (blk < 192 && tid < 64) ((v4u*)(a.ws + CTRL_OFF + blk * KB))[tid] = v4u{0u, 0u, 0u, 0u};
  v4u cpf = {};
  {
    unsigned long long pc; int d0; unsigned len;
    asm volatile("s_getpc_b64 %0\n.Lmk_prep:\n\ts_mov_b32 %1, .Lmk_head-.Lmk_prep\n\ts_mov_b32 %2, .Lmk_tail-.Lmk_head" : "=s"(pc), "=s"(d0), "=s"(len));
    const unsigned o = (unsigned)(blk >> 3) * 4096u + (unsigned)tid * 16u;
    if (o + 16 < len) cpf = *(const GAS v4u*)(((pc + (long long)d0) & ~15ull) + o);
  }
  int v = blk * 4 + (tid >> 6);
  if (v >= V_QKV + V_X) {
    v -= V_QKV + V_X;
    const int cc = lane & 7, h2 = (lane >> 3) & 1, kl = lane >> 4;
    const int pr = v >> 3, ksg = (v & 7) * 4 + kl, kq = ksg >> 3, nt = cc >> 2;
    const unsigned m2 = ((ksg & 1) && h2) ? 0u : ~0u;
    const unsigned short* src = a.wo + (size_t)(DH * (ksg >> 1) + 16 * (ksg & 1) + 4 * h2) * DM + 64 * pr + 8 * cc;
    v4u t[8];
#pragma unroll
    for (int j = 0; j < 8; ++j) t[j] = *(const v4u*)(src + (size_t)((j >= 4 && m2 ? 8 : 0) + (j & 3)) * DM);
    v4u* dst = (v4u*)(a.ws + WO_OFF + (((pr * 4 + kq) * 8 + (ksg & 7)) * 2 + (nt ^ (kq & 1))) * KB + (h2 * 32 + (cc & 3) * 8) * 16);
    dst[0] = v4u{plo(t[0].x, t[1].x), plo(t[2].x, t[3].x), plo(t[4].x, t[5].x) & m2, plo(t[6].x, t[7].x) & m2};
    dst[1] = v4u{phi(t[0].x, t[1].x), phi(t[2].x, t[3].x), phi(t[4].x, t[5].x) & m2, phi(t[6].x, t[7].x) & m2};
    dst[2] = v4u{plo(t[0].y, t[1].y), plo(t[2].y, t[3].y), plo(t[4].y, t[5].y) & m2, plo(t[6].y, t[7].y) & m2};
    dst[3] = v4u{phi(t[0].y, t[1].y), phi(t[2].y, t[3].y), phi(t[4].y, t[5].y) & m2, phi(t[6].y, t[7].y) & m2};
    dst[4] = v4u{plo(t[0].z, t[1].z), plo(t[2].z, t[3].z), plo(t[4].z, t[5].z) & m2, plo(t[6].z, t[7].z) & m2};
    dst[5] = v4u{phi(t[0].z, t[1].z), phi(t[2].z, t[3].z), phi(t[4].z, t[5].z) & m2, phi(t[6].z, t[7].z) & m2};
    dst[6] = v4u{plo(t[0].w, t[1].w), plo(t[2].w, t[3].w), plo(t[4].w, t[5].w) & m2, plo(t[6].w, t[7].w) & m2};
    dst[7] = v4u{phi(t[0].w, t[1].w), phi(t[2].w, t[3].w), phi(t[4].w, t[5].w) & m2, phi(t[6].w, t[7].w) & m2};
  } else if (v >= V_QKV) {
    v -= V_QKV;
    const int T = v / 7, j0 = (v % 7) * 2, r = lane & 31, hh = lane >> 5;
    const unsigned short* row = a.x + (size_t)(32 * T + r) * DM + 4 * hh + 32 * j0;
    v2u q[8];
#pragma unroll
    for (int i = 0; i < 8; ++i) q[i] = *(const v2u*)(row + 8 * i);
#pragma unroll
    for (int jj = 0; jj < 2; ++jj) {
      const v4u f0 = v4u{q[4 * jj].x, q[4 * jj].y, q[4 * jj + 1].x, q[4 * jj + 1].y}, f1 = v4u{q[4 * jj + 2].x, q[4 * jj + 2].y, q[4 * jj + 3].x, q[4 * jj + 3].y};
      *(v4u*)(a.ws + X0_OFF + (T * KS_D + 2 * (j0 + jj)) * KB + lane * 16) = f0;
      *(v4u*)(a.ws + X0_OFF + (T * KS_D + 2 * (j0 + jj) + 1) * KB + lane * 16) = f1;
      const f32x2 st = tile_stats(f0, f1);
      if (hh == 0) *(f32x2*)(a.ws + X0_OFF + XS + (T * 14 + j0 + jj) * 256 + r * 8) = st;
    }
  } else {
    const int cc = lane & 7, hh = (lane >> 3) & 1, kl = lane >> 4;
    const int tile = v / 7, ks = (v % 7) * 4 + kl, which = tile >> 4, head = tile & 15;
    const unsigned short* src = a.wqkv + (size_t)(16 * ks + 4 * hh) * (3 * DM) + which * DM + head * DH + (cc < 7 ? cc : 0) * 4;
    v2u t[8];
#pragma unroll
    for (int j = 0; j < 8; ++j) t[j] = *(const v2u*)(src + (size_t)(8 * (j >> 2) + (j & 3)) * (3 * DM));
    v4u* dst = (v4u*)(a.ws + WQKV_OFF + ((head * KS_D + ks) * 3 + which) * KB + (hh * 32 + cc * 4) * 16);
    const unsigned m = cc < 7 ? ~0u : 0u;
    const v4u o0 = v4u{plo(t[0].x, t[1].x) & m, plo(t[2].x, t[3].x) & m, plo(t[4].x, t[5].x) & m, plo(t[6].x, t[7].x) & m};
    const v4u o1 = v4u{phi(t[0].x, t[1].x) & m, phi(t[2].x, t[3].x) & m, phi(t[4].x, t[5].x) & m, phi(t[6].x, t[7].x) & m};
    const v4u o2 = v4u{plo(t[0].y, t[1].y) & m, plo(t[2].y, t[3].y) & m, plo(t[4].y, t[5].y) & m, plo(t[6].y, t[7].y) & m};
    const v4u o3 = v4u{phi(t[0].y, t[1].y) & m, phi(t[2].y, t[3].y) & m, phi(t[4].y, t[5].y) & m, phi(t[6].y, t[7].y) & m};
    dst[0] = o0; dst[1] = o1; dst[2] = o2; dst[3] = o3;
    const f32x4 cs = f32x4{sum8(o0), sum8(o1), sum8(o2), sum8(o3)};
    if (lane < 8) *(f32x4*)(a.ws + CSQ_OFF + (((v % 7) * 48 + tile) * 32 + cc * 4) * 4) = cs;
  }
  asm volatile("" ::"v"(cpf));
}

struct MainArgs { unsigned char* ws; unsigned short* out; const unsigned short *wg, *wu, *wd; };

__device__ __forceinline__ f32x16 reduce4(LAS unsigned char* L, const f32x16 (&acc)[4], int w, int lane) {
  LAS f32x4* red = (LAS f32x4*)L + lane;
#pragma unroll
  for (int d = 1; d < 4; ++d) {
    LAS f32x4* dst = red + (((w ^ d) * 3 + d - 1) * 4) * 64;
#pragma unroll
    for (int q = 0; q < 4; ++q) dst[q * 64] = f32x4{acc[d][4 * q], acc[d][4 * q + 1], acc[d][4 * q + 2], acc[d][4 * q + 3]};
  }
  f32x16 r = acc[0];
  wg_barrier();
  const LAS f32x4* src = red + w * 12 * 64;
#pragma unroll
  for (int j = 0; j < 3; ++j)
#pragma unroll
    for (int q = 0; q < 4; ++q) {
      f32x4 v = src[(j * 4 + q) * 64];
      r[4 * q] += v.x; r[4 * q + 1] += v.y; r[4 * q + 2] += v.z; r[4 * q + 3] += v.w;
    }
  return r;
}
__device__ __forceinline__ void add_res(const f32x16& y, v4u xr0, v4u xr1, v4u& o0, v4u& o1) {
  unsigned a0 = xr0.x, a1 = xr0.y, a2 = xr0.z, a3 = xr0.w, b0 = xr1.x, b1 = xr1.y, b2 = xr1.z, b3 = xr1.w;
  o0 = v4u{pack2(y[0] + lo_f(a0), y[1] + hi_f(a0)), pack2(y[2] + lo_f(a1), y[3] + hi_f(a1)),
           pack2(y[4] + lo_f(a2), y[5] + hi_f(a2)), pack2(y[6] + lo_f(a3), y[7] + hi_f(a3))};
  o1 = v4u{pack2(y[8] + lo_f(b0), y[9] + hi_f(b0)), pack2(y[10] + lo_f(b1), y[11] + hi_f(b1)),
           pack2(y[12] + lo_f(b2), y[13] + hi_f(b2)), pack2(y[14] + lo_f(b3), y[15] + hi_f(b3))};
}
struct StatsIn { v2u p[7]; };
__device__ __forceinline__ void stats_fin(const StatsIn& si, float& mean, float& var) {
  float s = 0.f, a, b;
#pragma unroll
  for (int j = 0; j < 7; ++j) { unsigned x = si.p[j].x; s += as_f(x); }
  halves(s, a, b);
  mean = (a + b) * (1.f / DM);
  float q = 0.f;
#pragma unroll
  for (int j = 0; j < 7; ++j) {
    unsigned x = si.p[j].x, y = si.p[j].y;
    const float d = as_f(x) * (1.f / 32.f) - mean;
    q += as_f(y) + 32.f * d * d;
  }
  halves(q, a, b);
  var = (a + b) * (1.f / DM) + 1e-5f;
}
#define LDSV(p) (*(const LAS v4u*)(p))
#define PACK8(t, o) v4u{pack2(t[o], t[o + 1]), pack2(t[o + 2], t[o + 3]), pack2(t[o + 4], t[o + 5]), pack2(t[o + 6], t[o + 7])}

constexpr int X0N = 14;
constexpr int XI = 14;
constexpr int SC = 7;
constexpr int WG_B = 14;
constexpr int HPUB = 64;
constexpr int HW = 9;
constexpr int PFA = 24, PFB = 21;
constexpr int pf_gap(int j) { return PFA + j * PFB / 10; }
constexpr int pf_index(int G) { for (int j = 0; j < 84; ++j) if (pf_gap(j) == G) return j; return -1; }
constexpr int pf_count(int G0, int G1) { int n = 0; for (int j = 0; j < 84; ++j) { const int G = pf_gap(j); if (G >= G0 && G < G1) ++n; } return n; }
constexpr bool pf_ok() { for (int j = 0; j < 84; ++j) if (pf_index(pf_gap(j)) != j || pf_gap(j) >= 8 * KS_D) return false; return true; }
static_assert(pf_ok(), "prefetch schedule");

__global__ void __launch_bounds__(256, 1) k_main(MainArgs a) {
  extern __shared__ __attribute__((aligned(16))) unsigned char smem_[];
  LAS unsigned char* L = (LAS unsigned char*)smem_;
  const int tid = threadIdx.x, lane = tid & 63, hh = lane >> 5, r = lane & 31;
  const int w = __builtin_amdgcn_readfirstlane(tid >> 6);
  const int bid = blockIdx.x;
  const int xcd = bid & 7, idx = bid >> 3;
  const int p = 4 * (xcd >> 1) + (idx >> 3), head = 8 * (xcd & 1) + (idx & 7);
  const bool gemm = idx < 28;
  const int i7 = idx / 7, j7 = idx - 7 * i7;
  const int gp = 4 * (xcd >> 1) + i7, slice = 7 * (xcd & 1) + j7;
  const int dp = 2 * xcd + (i7 >> 1), half = i7 & 1, pair = j7;
  const __amdgpu_buffer_rsrc_t rs = __builtin_amdgcn_make_buffer_rsrc((void*)a.ws, 0, WS_BYTES, RSRC_W3);
  const int l16 = lane * 16;
  const int svo = hh * 7 * 256 + r * 8;
  const int T = 4 * p + w;
  const int fmt = w >> 1, fnt = w & 1;
  const int T0 = 4 * dp + 2 * half;
  const int xres = ((T0 + fmt) * KS_D + 2 * (2 * pair + fnt)) * KB;
  const int xsts = XS + ((T0 + fmt) * 14 + 2 * pair + fnt) * 256;
  const int wqkv_src = WQKV_OFF + (head * 84 + w * 21) * KB;
  v4u cpf0 = {}, cpf1 = {};
  if (w == 3) {
    unsigned long long pc; unsigned len;
    asm volatile("s_getpc_b64 %0\n.Lmk_head:\n\ts_mov_b32 %1, .Lmk_tail-.Lmk_head" : "=s"(pc), "=s"(len));
    const unsigned o = (unsigned)idx * 1024u + (unsigned)l16;
    const GAS unsigned char* cb = (const GAS unsigned char*)(pc & ~15ull);
    if (o + 16 < len) cpf0 = *(const GAS v4u*)(cb + o);
    if (o + 32768 + 16 < len) cpf1 = *(const GAS v4u*)(cb + o + 32768);
  }
  const unsigned short* rt_src; int rt_ld;
  int rt_dst = 0, rt_flag = 0, rt_cs = 0, rt_ks = KB;
  {
    int v = bid + 256 * w;
    const int cc = lane & 7, h2 = (lane >> 3) & 1, kl = lane >> 4, ct = lane >> 3;
    const unsigned short* src = a.wd; int ld = DM;
    if (v < V_GU) {
      const int mat = v / 196, cg = (v % 196) / 7, ks0 = (v % 7) * 4, c0 = 64 * cg + 8 * ct;
      src = (mat ? a.wu : a.wg) + (size_t)(16 * (ks0 + kl) + 4 * h2) * FF + 64 * cg + 8 * cc; ld = FF;
      rt_dst = WGU_OFF + ((c0 >> 4) * KS_D + ks0) * KB + (mat * 16 + (c0 & 15) + cc) * 16;
      rt_flag = W_WT + (7 + (cg >> 1)) * 32 + (mat * 2 + (cg & 1)) * 7 + v % 7;
      rt_cs = CSG_OFF + (((v % 7) * 2 + mat) * FF + 64 * cg + lane) * 4;
    } else if (v < V_GU + V_WD) {
      v -= V_GU;
      const int pr = v / 28, ks0 = (v % 28) * 4, kq = ks0 / KS_D;
      src = a.wd + (size_t)(16 * (ks0 + kl) + 4 * h2) * DM + 64 * pr + 8 * cc;
      rt_dst = WD_OFF + (((pr * 4 + kq) * KS_D + ks0 % KS_D) * 2 + ((ct >> 2) ^ (kq & 1))) * KB + ((ct & 3) * 8 + cc) * 16;
      rt_flag = W_WT + (21 + pr) * 32 + v % 28;
      rt_ks = 2 * KB;
    }
    rt_src = src; rt_ld = ld;
  }
  const bool rt_on = bid + 256 * w < V_GU + V_WD;

  auto body = [&](auto lc) __attribute__((always_inline)) -> bool {
    constexpr int layer = decltype(lc)::v;
    const int LO = LAYER_OFF + layer * LAYER_BYTES;
    const int xin = layer == 0 ? X0_OFF : LAYER_OFF + X2_L;
    v4u wo[16], wg[2 * KS_D];
    unsigned fwt = 0;
    const int wt_vo = (lane < 28 ? (7 + slice) * 32 + lane : lane < 56 ? (21 + pair) * 32 + lane - 28 : (7 + slice) * 32) * 4;
    {
      v4u xa[KS_D], raw[8];
      StatsIn si;
      const int bx = opq(xin + T * KS_D * KB), bxs = opq(xin + XS + T * 14 * 256);
      {
        if constexpr (layer == 0) {
          v4u tq[21];
          const int bq = opq(wqkv_src);
          unsigned csp[7];
#pragma unroll
          for (int i = 0; i < 7; ++i)
            csp[i] = __builtin_amdgcn_raw_buffer_load_b32(rs, tid < 96 ? ((tid >> 5) * 16 * 32 + (tid & 31)) * 4 : WS_BYTES, CSQ_OFF + (i * 48 + head) * 128, 0);
#pragma unroll
          for (int i = 0; i < 21; ++i) tq[i] = ldf_pl(rs, l16, bq, i);
#pragma unroll
          for (int k = 0; k < X0N; ++k) xa[k] = ldf_sc1(rs, l16, bx, k);
          SCHED();
#pragma unroll
          for (int i = 0; i < 21; ++i) *(LAS v4u*)(L + L_A + (w * 21 + i) * KB + l16) = tq[i];
          float cs0 = 0.f;
#pragma unroll
          for (int i = 0; i < 7; ++i) cs0 += as_f(csp[i]);
          if (tid < 96) ((LAS float*)(L + L_CS))[tid] = cs0;
          asm volatile("" ::"v"(cpf0), "v"(cpf1));
          wg_barrier();
        } else {
          wg_barrier();
          if (!wait_flags(rs, W_X2 + T * 32, 14, lane)) return false;
#pragma unroll
          for (int k = 0; k < X0N; ++k) xa[k] = ldf_sc1(rs, l16, bx, k);
          SCHED();
        }
      }
      const LAS unsigned char* wb0 = L + L_A + l16;
      const LAS unsigned char* wb1 = L + L_A + 42 * KB + l16;
#define WQ(k, which) LDSV(((k) < 14 ? wb0 : wb1) + (((k) % 14) * 3 + (which)) * KB)
      f32x16 ak = {}, av = {}, aq = {};
      float mean = 0.f, rstd = 0.f;
      LAS f32x2* st = (LAS f32x2*)(L + L_ST) + w * 32;
      const LAS float* cs = (const LAS float*)(L + L_CS);
      const int kvo = LO + KV_L + (head * 64) * 4 * KB;
      const int bkv = opq(kvo + T * 4 * KB);
      LAS unsigned char* kvs = L + L_KV + (8 + w) * 4 * KB + l16;
      v4u qf[2];
      {
        v4u wf[6];
        float t[16], cv = 0.f;
        f32x4 c4[4], s01[4], s23[4];
#define WSEQ(i) ((i) < 2 * KS_D ? WQ((i) >> 1, 1 + ((i) & 1)) : WQ((i) - 2 * KS_D, 0))
        wf[0] = WSEQ(0); wf[1] = WSEQ(1); wf[2] = WSEQ(2); wf[3] = WSEQ(3); wf[4] = WSEQ(4);
        SFOR(i, 0, 3 * KS_D)
          constexpr int q0 = 2 * KS_D;
          if constexpr (i >= q0) aq = mfma(wf[i % 6], xa[i - q0], aq);
          else if constexpr (i & 1) av = mfma(xa[i >> 1], wf[i % 6], av);
          else ak = mfma(wf[i % 6], xa[i >> 1], ak);
          SCHED();
          if constexpr (i + 5 < 3 * KS_D) wf[(i + 5) % 6] = WSEQ(i + 5);
          if constexpr (X0N + i < KS_D) xa[X0N + i] = ldf_sc1(rs, l16, bx, X0N + i);
          else if constexpr (X0N + i < KS_D + 7) si.p[X0N + i - KS_D] = __builtin_amdgcn_raw_buffer_load_b64(rs, svo + (X0N + i - KS_D) * 256, bxs, AUX_SC1);
          if constexpr (layer == 0) { if constexpr (i >= 30 && i < 38) raw[i - 30] = *(const v4u*)(rt_src + (size_t)((i - 30 >= 4 ? 8 : 0) + ((i - 30) & 3)) * rt_ld); }
          if constexpr (i == q0 - 2) {
            stats_fin(si, mean, rstd);
            rstd = __builtin_amdgcn_rsqf(rstd);
            if (hh == 0) st[r] = f32x2{mean, rstd};
            cv = cs[64 + r];
          }
          if constexpr (i >= q0 - 10 && i < q0 - 6) c4[i - q0 + 10] = *(const LAS f32x4*)(cs + 32 + 8 * (i - q0 + 10) + 4 * hh);
          if constexpr (i >= q0 && i < q0 + 4) { s01[i - q0] = *(const LAS f32x4*)(st + 8 * (i - q0) + 4 * hh); s23[i - q0] = *(const LAS f32x4*)(st + 8 * (i - q0) + 4 * hh + 2); }
          if constexpr (i >= q0 + 1 && i < q0 + 5) {
            constexpr int g = i - q0 - 1;
            t[4 * g + 0] = rstd * (ak[4 * g + 0] - mean * c4[g].x); t[4 * g + 1] = rstd * (ak[4 * g + 1] - mean * c4[g].y);
            t[4 * g + 2] = rstd * (ak[4 * g + 2] - mean * c4[g].z); t[4 * g + 3] = rstd * (ak[4 * g + 3] - mean * c4[g].w);
          }
          if constexpr (i == q0 + 5) { v4u f = PACK8(t, 0); stf_sc1(f, rs, l16, bkv, 0); *(LAS v4u*)(kvs) = f; }
          if constexpr (i == q0 + 6) {
            v4u f = PACK8(t, 8); stf_sc1(f, rs, l16, bkv, 1); *(LAS v4u*)(kvs + KB) = f;
          }
          if constexpr (i >= q0 + 7 && i < q0 + 11) {
            constexpr int g = i - q0 - 7;
            t[4 * g + 0] = s01[g].y * (av[4 * g + 0] - s01[g].x * cv); t[4 * g + 1] = s01[g].w * (av[4 * g + 1] - s01[g].z * cv);
            t[4 * g + 2] = s23[g].y * (av[4 * g + 2] - s23[g].x * cv); t[4 * g + 3] = s23[g].w * (av[4 * g + 3] - s23[g].z * cv);
          }
          if constexpr (i == q0 + 11 || i == q0 + 12) {
            constexpr int o8 = (i - q0 - 11) * 8;
            v4u f = PACK8(t, o8);
            if (r == DH) f = v4u{0x3f803f80u, 0x3f803f80u, 0x3f803f80u, 0x3f803f80u};
            stf_sc1(f, rs, l16, bkv, 2 + (o8 >> 3)); *(LAS v4u*)(kvs + (2 + (o8 >> 3)) * KB) = f;
          }
          SCHED();
        SEND
        publish(rs, W_KV + ((layer * 16 + head) * 16 + p) * 32 + w, lane);
      }
      const int bo = opq(gemm ? WO_OFF + (pair * 4 + w) * 16 * KB : WS_BYTES);
      {
        const int pp = p - 2 + (w >> 1);
#pragma unroll
        for (int i = 0; i < 16; ++i) wo[i] = ldf_pl(rs, l16, bo, i);
        SCHED();
        {
          const float qs = rstd * (0.18898223650461362f * 1.4426950408889634f);
          float q[16];
#pragma unroll
          for (int g = 0; g < 4; ++g) {
            f32x4 c = *(const LAS f32x4*)(cs + 8 * g + 4 * hh);
            q[4 * g + 0] = qs * (aq[4 * g + 0] - mean * c.x); q[4 * g + 1] = qs * (aq[4 * g + 1] - mean * c.y);
            q[4 * g + 2] = qs * (aq[4 * g + 2] - mean * c.z); q[4 * g + 3] = qs * (aq[4 * g + 3] - mean * c.w);
          }
          if (hh) q[12] = 1.f;
          qf[0] = PACK8(q, 0); qf[1] = PACK8(q, 8);
        }
        SCHED();
        v4u t[8];
        if (pp >= 0) {
          if (!wait_flags(rs, W_KV + ((layer * 16 + head) * 16 + pp) * 32 + 2 * (w & 1), 2, lane)) return false;
          const int b = opq(kvo + (4 * p - 8 + 2 * w) * 4 * KB);
#pragma unroll
          for (int i = 0; i < 8; ++i) t[i] = ldf_sc1(rs, l16, b, i);
        } else {
#pragma unroll
          for (int i = 0; i < 8; ++i) t[i] = v4u{0u, 0u, ((i & 3) == 1 && hh) ? 0xF149u : 0u, 0u};
        }
        SCHED();
        SCHED();
#pragma unroll
        for (int i = 0; i < 8; ++i) *(LAS v4u*)(L + L_KV + (2 * w * 4 + i) * KB + l16) = t[i];
      }
      wg_barrier();
      f32x16 s[9];
      const LAS unsigned char* kvb = L + L_KV + w * 4 * KB + l16;
      float mx = -__builtin_inff();
      {
#define TORD(t) ((t) == 0 ? 0 : (t) == 1 ? 8 : (t) - 1)
        v4u k0[2], k1[2];
        k0[0] = LDSV(kvb); k1[0] = LDSV(kvb + KB);
#pragma unroll
        for (int t = 0; t < 10; ++t) {
          if (t + 1 < 9) { k0[(t + 1) & 1] = LDSV(kvb + TORD(t + 1) * 4 * KB); k1[(t + 1) & 1] = LDSV(kvb + TORD(t + 1) * 4 * KB + KB); }
          SCHED();
          if (t < 9) {
            asm("v_mfma_f32_32x32x16_bf16 %0, %1, %2, 0" : "=&v"(s[TORD(t)]) : "v"(k0[t & 1]), "v"(qf[0]));
            asm("v_mfma_f32_32x32x16_bf16 %0, %1, %2, %0" : "+v"(s[TORD(t)]) : "v"(k1[t & 1]), "v"(qf[1]));
          } else {
            asm volatile("s_nop 7\n\ts_nop 7\n\ts_nop 7" : "+v"(s[7]));
          }
          SCHED();
          if (t == 2) {
#pragma unroll
            for (int i = 0; i < 16; ++i) {
              const int kl = (i & 3) + 8 * (i >> 2) + 4 * hh;
              s[8][i] = (kl > r) ? s[0][i] : s[8][i];
            }
          }
          if (t >= 2) {
#pragma unroll
            for (int i = 0; i < 16; ++i) mx = fmaxf(mx, s[TORD(t - 1)][i]);
          }
          SCHED();
        }
      }
      { float lo, hi; halves(mx, lo, hi); mx = fmaxf(lo, hi); }
      f32x16 o = {};
#pragma unroll
      for (int t = 1; t < 9; ++t) {
        v4u v0 = LDSV(kvb + t * 4 * KB + 2 * KB), v1 = LDSV(kvb + t * 4 * KB + 3 * KB), u0, u1;
        if (t == 8) { u0 = LDSV(kvb + 2 * KB); u1 = LDSV(kvb + 3 * KB); }
        SCHED();
        float e[16];
#pragma unroll
        for (int i = 0; i < 16; ++i) e[i] = __builtin_amdgcn_exp2f(s[t][i] - mx);
        v4u p0 = PACK8(e, 0), p1 = PACK8(e, 8);
        if (t == 8) {
          unsigned mk[8];
#pragma unroll
          for (int d = 0; d < 8; ++d) {
            const int kl = ((2 * d) & 3) + 8 * (d >> 1) + 4 * hh;
            mk[d] = (kl > r ? 0xffffu : 0u) | (kl + 1 > r ? 0xffff0000u : 0u);
          }
          o = mfma(u0, v4u{p0.x & mk[0], p0.y & mk[1], p0.z & mk[2], p0.w & mk[3]}, o);
          o = mfma(u1, v4u{p1.x & mk[4], p1.y & mk[5], p1.z & mk[6], p1.w & mk[7]}, o);
          p0 = v4u{p0.x & ~mk[0], p0.y & ~mk[1], p0.z & ~mk[2], p0.w & ~mk[3]};
          p1 = v4u{p1.x & ~mk[4], p1.y & ~mk[5], p1.z & ~mk[6], p1.w & ~mk[7]};
        }
        o = mfma(v0, p0, o);
        o = mfma(v1, p1, o);
        SCHED();
      }
      {
        float lo, hi; halves(o[12], lo, hi);
        const float inv = __builtin_amdgcn_rcpf(hi);
        float t[16];
#pragma unroll
        for (int i = 0; i < 16; ++i) t[i] = o[i] * inv;
        if (hh) { t[12] = 0.f; t[13] = 0.f; t[14] = 0.f; t[15] = 0.f; }
        const int b = opq(LO + O_L + (T * KS_O + 2 * head) * KB);
        stf_sc1(PACK8(t, 0), rs, l16, b, 0); stf_sc1(PACK8(t, 8), rs, l16, b, 1);
      }
      publish(rs, W_OF + ((layer * 32 + (T >> 1)) * 4 + (head >> 2)) * 32 + (T & 1) * 4 + (head & 3), lane);
      if constexpr (layer == 0) if (rt_on) {
        v4u o[8];
        o[0] = v4u{plo(raw[0].x, raw[1].x), plo(raw[2].x, raw[3].x), plo(raw[4].x, raw[5].x), plo(raw[6].x, raw[7].x)};
        o[1] = v4u{phi(raw[0].x, raw[1].x), phi(raw[2].x, raw[3].x), phi(raw[4].x, raw[5].x), phi(raw[6].x, raw[7].x)};
        o[2] = v4u{plo(raw[0].y, raw[1].y), plo(raw[2].y, raw[3].y), plo(raw[4].y, raw[5].y), plo(raw[6].y, raw[7].y)};
        o[3] = v4u{phi(raw[0].y, raw[1].y), phi(raw[2].y, raw[3].y), phi(raw[4].y, raw[5].y), phi(raw[6].y, raw[7].y)};
        o[4] = v4u{plo(raw[0].z, raw[1].z), plo(raw[2].z, raw[3].z), plo(raw[4].z, raw[5].z), plo(raw[6].z, raw[7].z)};
        o[5] = v4u{phi(raw[0].z, raw[1].z), phi(raw[2].z, raw[3].z), phi(raw[4].z, raw[5].z), phi(raw[6].z, raw[7].z)};
        o[6] = v4u{plo(raw[0].w, raw[1].w), plo(raw[2].w, raw[3].w), plo(raw[4].w, raw[5].w), plo(raw[6].w, raw[7].w)};
        o[7] = v4u{phi(raw[0].w, raw[1].w), phi(raw[2].w, raw[3].w), phi(raw[4].w, raw[5].w), phi(raw[6].w, raw[7].w)};
        LAS unsigned char* sc = L + L_RT + w * 8 * KB;
#pragma unroll
        for (int j = 0; j < 8; ++j) *(LAS v4u*)(sc + lane * 128 + ((j ^ lane) & 7) * 16) = o[j];
        asm volatile("s_waitcnt lgkmcnt(0)" ::: "memory");
        float cj = 0.f;
#pragma unroll
        for (int j = 0; j < 8; ++j) {
          const int sl = j * 8 + (lane >> 3);
          const v4u pc = LDSV(sc + sl * 128 + ((lane ^ sl) & 7) * 16);
          st_sc1(pc, rs, rt_dst + (j & 1) * 512 + (j >> 1) * rt_ks, 0);
          unsigned x = pc.x, y = pc.y, z = pc.z, u = pc.w;
          cj += ((lo_f(x) + hi_f(x)) + (lo_f(y) + hi_f(y))) + ((lo_f(z) + hi_f(z)) + (lo_f(u) + hi_f(u)));
        }
        if (bid + 256 * w < V_GU) __builtin_amdgcn_raw_buffer_store_b32(as_u(cj), rs, rt_cs, 0, AUX_SC1);
      }
      if constexpr (layer == 0) if (rt_on && !gemm) publish(rs, __builtin_amdgcn_readfirstlane(rt_flag), lane);
    }
    if (!gemm) return true;
    const int bg = opq(WGU_OFF + (slice * 4 + w) * 2 * KS_D * KB);
    {
      const int bxr = opq(xin + xres);
      v4u xr0 = ldf_sc1(rs, l16, bxr, 0), xr1 = ldf_sc1(rs, l16, bxr, 1);
      if constexpr (layer > 0) {
#pragma unroll
        for (int i = 0; i < WG_B; ++i) wg[i] = ldf_sc1(rs, l16, bg, i);
      }
      SCHED();
      if (!wait_flags(rs, W_OF + ((layer * 32 + 2 * dp + half) * 4 + w) * 32, 8, lane)) return false;
      if constexpr (layer == 0) if (rt_on) publish(rs, __builtin_amdgcn_readfirstlane(rt_flag), lane);
      v4u oa[2][8];
      const int bo0 = opq(LO + O_L + ((T0 + fmt) * KS_O + 8 * w) * KB), bo1 = opq(LO + O_L + ((T0 + (fmt ^ 1)) * KS_O + 8 * w) * KB);
#pragma unroll
      for (int k = 0; k < 8; ++k) { oa[0][k] = ldf_sc1(rs, l16, bo0, k); oa[1][k] = ldf_sc1(rs, l16, bo1, k); }
      SCHED();

      f32x16 acc[4] = {};
#pragma unroll
      for (int k = 0; k < 8; ++k) {
        acc[0] = mfma(wo[2 * k], oa[0][k], acc[0]); acc[1] = mfma(wo[2 * k + 1], oa[0][k], acc[1]);
        acc[2] = mfma(wo[2 * k], oa[1][k], acc[2]); acc[3] = mfma(wo[2 * k + 1], oa[1][k], acc[3]);
      }
      f32x16 y = reduce4(L + L_A, acc, w, lane);
      v4u o0, o1; add_res(y, xr0, xr1, o0, o1);
      const int bs = opq(LO + X1_L + xres);
      stf_sc1(o0, rs, l16, bs, 0); stf_sc1(o1, rs, l16, bs, 1);
      const f32x2 ts = tile_stats(o0, o1);
      if (hh == 0) __builtin_amdgcn_raw_buffer_store_b64(v2u{as_u(ts.x), as_u(ts.y)}, rs, r * 8, opq(LO + X1_L + xsts), AUX_SC1);
      if constexpr (layer == 0) fwt = __builtin_amdgcn_raw_buffer_load_b32(rs, wt_vo, opq(CTRL_OFF + W_WT * 4), AUX_SC1);
      publish(rs, W_X1 + (layer * 64 + T0 + fmt) * 32 + 2 * pair + fnt, lane);
    }
    v4u wd[2 * KS_D];
    const int bd = opq(WD_OFF + (pair * 4 + w) * 2 * KS_D * KB);
    {
      unsigned cgp[7];
      if constexpr (layer == 0) {
        if (__builtin_amdgcn_ballot_w64(fwt == 0u) != 0ull) { if (!wait_flags_v(rs, W_WT, wt_vo)) return false; }
        asm volatile("" ::: "memory");
        __builtin_amdgcn_fence(__ATOMIC_ACQUIRE, "wavefront");
#pragma unroll
        for (int i = 0; i < 7; ++i)
          cgp[i] = __builtin_amdgcn_raw_buffer_load_b32(rs, (((tid >> 4) & 1) * FF + slice * 128 + (tid >> 5) * 16 + (tid & 15)) * 4, CSG_OFF + i * 2 * FF * 4, AUX_SC1);
#pragma unroll
        for (int i = 0; i < WG_B; ++i) wg[i] = ldf_sc1(rs, l16, bg, i);
      }
#pragma unroll
      for (int i = WG_B; i < KS_D; ++i) wg[i] = ldf_sc1(rs, l16, bg, i);
      SCHED();
      const int bx = opq(LO + X1_L + (4 * gp + w) * KS_D * KB);
      LAS unsigned char* dst = L + L_A + w * KS_D * KB + l16;
      v4u xa[KS_D];
      StatsIn si;
      wg_barrier();
      if (!wait_flags(rs, W_X1 + (layer * 64 + 4 * gp + w) * 32, 14, lane)) return false;
      if constexpr (layer == 0) {
        float c1 = 0.f;
#pragma unroll
        for (int i = 0; i < 7; ++i) c1 += as_f(cgp[i]);
        ((LAS float*)(L + L_CS))[96 + tid] = c1;
      }
#pragma unroll
      for (int k = 0; k < XI; ++k) xa[k] = ldf_sc1(rs, l16, bx, k);
      const int bxs1 = opq(LO + X1_L + XS + (4 * gp + w) * 14 * 256);
      SCHED();

#pragma unroll
      for (int k = 0; k < SC; ++k) *(LAS v4u*)(dst + k * KB) = xa[k];
      wg_barrier();
      const LAS float* cs = (const LAS float*)(L + L_CS) + 96 + w * 64;
      const int bh = opq(LO + H_L + (4 * gp * KS_F + 8 * slice + 2 * w) * KB);
      const int gw = 4 * slice + w;
      const int hfw = W_HF + ((layer * 16 + gp) * 8 + gw / 14) * 32 + gw % 14;
      const LAS unsigned char* xb0 = L + L_A + l16;
      const LAS unsigned char* xb1 = L + L_A + 2 * KS_D * KB + l16;
#define XB_(mt, k) LDSV(((mt) < 2 ? xb0 : xb1) + (((mt) & 1) * KS_D + (k)) * KB)
      float mean[4], r2[4], hv[8];
      f32x4 c[2][4];
      f32x16 a0[4] = {}, a1[4] = {};
      v4u xb[2][4];
#define PF_GAP(G) do { constexpr int j_ = pf_index(G); \
        if constexpr (j_ >= 0 && j_ < KS_D) wg[KS_D + j_] = ldf_sc1(rs, l16, bg, KS_D + j_); \
        if constexpr (j_ >= KS_D) wd[j_ - KS_D] = ldf_sc1(rs, l16, bd, j_ - KS_D); } while (0)
#define EPI(ACC, tt, mt, e) do { constexpr int j_ = (e); \
        if constexpr (j_ < 8) hv[j_] = (ACC[mt][j_] - mean[mt] * c[tt][j_ >> 2][j_ & 3]) * ((ACC[mt][8 + j_] - mean[mt] * c[tt][2 + (j_ >> 2)][j_ & 3]) * r2[mt]); \
        else stf_sc1(PACK8(hv, 0), rs, l16, bh, (mt) * KS_F + (tt)); } while (0)
#pragma unroll
      for (int mt = 0; mt < 4; ++mt) xb[0][mt] = XB_(mt, 0);
      SFOR(k, 0, KS_D)
        if constexpr (k == 13) {
          float mn, vr; stats_fin(si, mn, vr);
          if (hh == 0) ((LAS f32x2*)(L + L_ST))[w * 32 + r] = f32x2{mn, __builtin_amdgcn_rcpf(vr)};
        }
        if constexpr ((k + 1) % SC == 0 && k + 1 < KS_D) wg_barrier();
        SFOR(mt, 0, 4)
          a0[mt] = mfma(wg[k], xb[k & 1][mt], a0[mt]);
          SCHED();
          if constexpr (k + 1 < KS_D) xb[(k + 1) & 1][mt] = XB_(mt, k + 1);
          else xb[0][mt] = XB_(mt, 0);
          if constexpr (XI + 4 * k + mt < KS_D) xa[XI + 4 * k + mt] = ldf_sc1(rs, l16, bx, XI + 4 * k + mt);
          else if constexpr (XI + 4 * k + mt < KS_D + 7) si.p[XI + 4 * k + mt - KS_D] = __builtin_amdgcn_raw_buffer_load_b64(rs, svo + (XI + 4 * k + mt - KS_D) * 256, bxs1, AUX_SC1);
          {
            constexpr int kk = k + 1, c1 = kk / SC + 1, j = 2 * (kk % SC) + (mt >> 1) - (kk < SC ? 2 : 0);
            if constexpr ((mt & 1) && j >= 0 && j < SC && c1 * SC + j < KS_D) *(LAS v4u*)(dst + (c1 * SC + j) * KB) = xa[c1 * SC + j];
          }
          if constexpr (k == 21) { f32x2 u = ((const LAS f32x2*)(L + L_ST))[mt * 32 + r]; mean[mt] = u.x; r2[mt] = u.y; }
          if constexpr (k == 22) c[0][mt] = *(const LAS f32x4*)(cs + 8 * mt + 4 * hh);
          PF_GAP(4 * k + mt);
          SCHED();
        SEND
      SEND
      SFOR(g, 0, 4 * KS_D)
        constexpr int k = g >> 2, mt = g & 3;
        a1[mt] = mfma(wg[KS_D + k], xb[k & 1][mt], a1[mt]);
        SCHED();
        if constexpr (k + 1 < KS_D) xb[(k + 1) & 1][mt] = XB_(mt, k + 1);
        if constexpr (g == HPUB) {
          asm volatile("s_waitcnt vmcnt(%0)" ::"i"(pf_count(4 * KS_D + 36, 4 * KS_D + HPUB)) : "memory");
          raise_flag(rs, hfw, lane);
        }
        if constexpr (g < 36) EPI(a0, 0, g / 9, g % 9);
        if constexpr (g >= 100 && g < 104) c[1][g - 100] = *(const LAS f32x4*)(cs + 32 + 8 * (g - 100) + 4 * hh);
        PF_GAP(4 * KS_D + g);
        SCHED();
      SEND
      SFOR(e, 0, 36) EPI(a1, 1, e / 9, e % 9); SEND
      SCHED();
      publish(rs, hfw + 4 * 32, lane);
    }
    {
      const int bxr = opq(LO + X1_L + xres);
      v4u xr0 = ldf_sc1(rs, l16, bxr, 0), xr1 = ldf_sc1(rs, l16, bxr, 1);
      SCHED();
      const int hf = W_HF + ((layer * 16 + dp) * 8 + w) * 32;
      if (!wait_flags(rs, hf, 14, lane)) return false;
      f32x16 acc[4] = {};
      const int hb0 = opq(LO + H_L + ((T0 + fmt) * KS_F + KS_D * w) * KB), hb1 = opq(LO + H_L + ((T0 + (fmt ^ 1)) * KS_F + KS_D * w) * KB);
#define KO(i) ((i) < 14 ? 2 * (i) : 2 * ((i) - 14) + 1)
      v4u ha[2][HW], tq[21];
      const int bq = opq(wqkv_src);
      unsigned f1 = 0;
#define ISS(i) ((i) < 0 ? 9 : (i) < 5 ? 10 + (i) : ((i) + 1 + HW < KS_D ? (i) + 1 + HW : KS_D))
#pragma unroll
      for (int i = 0; i < 9; ++i) { ha[0][i] = ldf_sc1(rs, l16, hb0, KO(i)); ha[1][i] = ldf_sc1(rs, l16, hb1, KO(i)); }
      SCHED();
#pragma unroll
      for (int i = 0; i < KS_D; ++i) {
        const v4u h0 = ha[0][i % HW], h1 = ha[1][i % HW];
        if (i == 1) f1 = __builtin_amdgcn_raw_buffer_load_b32(rs, lane < 14 ? lane * 4 : 0, opq(CTRL_OFF + (hf + 4 * 32) * 4), AUX_SC1);
        if (i == 5) {
          if (__builtin_amdgcn_ballot_w64(f1 == 0u) != 0ull) { if (!wait_flags(rs, hf + 4 * 32, 14, lane)) return false; }
          asm volatile("" ::: "memory");
          __builtin_amdgcn_fence(__ATOMIC_ACQUIRE, "wavefront");
        }
        acc[0] = mfma(wd[2 * KO(i)], h0, acc[0]); acc[1] = mfma(wd[2 * KO(i) + 1], h0, acc[1]);
        SCHED();
#pragma unroll
        for (int j = ISS(i - 1); j < ISS(i); ++j) ha[0][j % HW] = ldf_sc1(rs, l16, hb0, KO(j));
        SCHED();
        acc[2] = mfma(wd[2 * KO(i)], h1, acc[2]); acc[3] = mfma(wd[2 * KO(i) + 1], h1, acc[3]);
        SCHED();
#pragma unroll
        for (int j = ISS(i - 1); j < ISS(i); ++j) ha[1][j % HW] = ldf_sc1(rs, l16, hb1, KO(j));
        if constexpr (layer == 0) {
          if (i >= 19) {
#pragma unroll
            for (int j = 0; j < 3; ++j) if (3 * (i - 19) + j < 21) tq[3 * (i - 19) + j] = ldf_pl(rs, l16, bq, 3 * (i - 19) + j);
          }
        }
        SCHED();
      }
      wg_barrier();
      f32x16 y = reduce4(L + L_KV, acc, w, lane);
      v4u o0, o1; add_res(y, xr0, xr1, o0, o1);
      if constexpr (layer == 0) {
        const int bs = opq(LO + X2_L + xres);
        stf_sc1(o0, rs, l16, bs, 0); stf_sc1(o1, rs, l16, bs, 1);
        const f32x2 ts = tile_stats(o0, o1);
        if (hh == 0) __builtin_amdgcn_raw_buffer_store_b64(v2u{as_u(ts.x), as_u(ts.y)}, rs, r * 8, opq(LO + X2_L + xsts), AUX_SC1);
        publish(rs, W_X2 + (T0 + fmt) * 32 + 2 * pair + fnt, lane);
#pragma unroll
        for (int i = 0; i < 21; ++i) *(LAS v4u*)(L + L_A + (w * 21 + i) * KB + l16) = tq[i];
      } else {
        unsigned short* orow = a.out + (size_t)(32 * (T0 + fmt) + r) * DM + 32 * (2 * pair + fnt) + 4 * hh;
        *(v2u*)(orow) = v2u{o0.x, o0.y}; *(v2u*)(orow + 8) = v2u{o0.z, o0.w};
        *(v2u*)(orow + 16) = v2u{o1.x, o1.y}; *(v2u*)(orow + 24) = v2u{o1.z, o1.w};
      }
    }
    if constexpr (layer == 1) asm volatile(".Lmk_tail:");
    return true;
  };
  if (!body(IC<0>{})) return;
  body(IC<1>{});
}
}

extern "C" void kernel_launch(void* const* d_in, const int* in_sizes, int n_in,
                              void* d_out, int out_size, void* d_ws, size_t ws_size,
                              hipStream_t stream) {
  PrepArgs pa;
  pa.x = (const unsigned short*)d_in[0];
  pa.wqkv = (const unsigned short*)d_in[1];
  pa.wo = (const unsigned short*)d_in[2];
  pa.ws = (unsigned char*)d_ws;
  (void)hipFuncSetAttribute((const void*)k_main, hipFuncAttributeMaxDynamicSharedMemorySize, L_BYTES);
  k_prep<<<PREP_UNITS, 256, 0, stream>>>(pa);
  MainArgs ma;
  ma.ws = (unsigned char*)d_ws; ma.out = (unsigned short*)d_out;
  ma.wg = (const unsigned short*)d_in[4];
  ma.wu = (const unsigned short*)d_in[5]; ma.wd = (const unsigned short*)d_in[6];
  k_main<<<256, 256, L_BYTES, stream>>>(ma);
}
```
